```python
import torch

def _softmax(x, axis=-1):
    return torch.softmax(x, dim=axis)
def _gelu(x):
    return torch.nn.functional.gelu(x, approximate="tanh")
def _silu(x):
    return torch.nn.functional.silu(x)
def _sigmoid(x):
    return torch.sigmoid(x)
def _layernorm(x, axis=-1, eps=1e-5):
    m = x.mean(dim=axis, keepdim=True)
    v = x.var(dim=axis, keepdim=True, unbiased=False)
    return (x - m) / torch.sqrt(v + eps)
def _scatter_add(tgt, idx, src, axis=0):
    return tgt.clone().index_add_(axis, idx, src)
def _segment_sum(src, seg, n):
    out = torch.zeros((n,) + src.shape[1:], dtype=src.dtype)
    return out.index_add_(0, seg, src)
def _pad(x, pw):
    flat = tuple(v for pair in reversed(pw) for v in pair)
    return torch.nn.functional.pad(x, flat)

kernel_name = 'standalone__sliding_local_strided__prompt'

def setup_inputs():

    _w = (lambda a: (lambda t: t.to(torch.bfloat16) if t.is_floating_point() else t)(torch.as_tensor(a)))
    B, H, S, D = 1, 32, 2048, 192
    return {
        "q": _w((lambda *s: torch.randn(*s, dtype=torch.bfloat16) * 0.8)(B, H, S, D)),
        "k": _w((lambda *s: torch.randn(*s, dtype=torch.bfloat16) * 0.8)(B, H, S, D)),
        "v": _w((lambda *s: torch.randn(*s, dtype=torch.bfloat16) * 0.05)(B, H, S, D)),
    }

def reference(q, k, v):
    B, H, S, D = q.shape
    W, dil = 8, 4
    scale = D ** -0.5
    pos = torch.arange(S)
    kidx = pos[:, None] - torch.arange(W)[None, :] * dil
    valid = kidx >= 0
    kidx = torch.where(valid, kidx, torch.zeros_like(kidx))
    idx = torch.reshape(kidx, (1, 1, S * W, 1))
    idx = (lambda x, n, axis: torch.repeat_interleave(x, n, dim=axis))(idx, B, 0)
    idx = (lambda x, n, axis: torch.repeat_interleave(x, n, dim=axis))(idx, H, 1)
    idx = (lambda x, n, axis: torch.repeat_interleave(x, n, dim=axis))(idx, D, 3)
    kg = torch.reshape((lambda x, i, axis: torch.take_along_dim(x, i, dim=axis))(k, idx, 2), (B, H, S, W, D))
    vg = torch.reshape((lambda x, i, axis: torch.take_along_dim(x, i, dim=axis))(v, idx, 2), (B, H, S, W, D))
    scores = torch.einsum("bhsd,bhswd->bhsw", q, kg) * scale
    mask_f = (lambda x, dt: x.to(dt))(valid, q.dtype)[None, None, :, :]
    scores = scores + (mask_f - 1.0) * 1e9
    p = _softmax(scores, axis=-1)
    return torch.einsum("bhsw,bhswd->bhsd", p, vg)
```

```cpp
#include <hip/hip_runtime.h>
#include <hip/hip_bf16.h>
#include <stdint.h>

#define CFG_AUXL 2
#define CFG_AUXH 0
#define CFG_AUXO 16
#define CFG_XCD 32
#define CFG_OCC 2
#define CFG_SHARE 0

typedef unsigned u32;
typedef __attribute__((ext_vector_type(4))) unsigned u32x4;
typedef __attribute__((ext_vector_type(2))) unsigned u32x2;
typedef __attribute__((ext_vector_type(2))) __bf16 bf16x2;
typedef __attribute__((ext_vector_type(8))) __bf16 bf16x8;
typedef __attribute__((ext_vector_type(2))) float f32x2;
typedef __attribute__((ext_vector_type(4))) float f32x4;

constexpr int NH = 32, NS = 2048, ND = 192;
constexpr int ROWB = ND * 2;
constexpr int SROWB = 4 * ROWB;
constexpr int HEADB = NS * ROWB;
constexpr u32 TENSORB = (u32)NH * HEADB;
constexpr int NBLK = NH * (NS / 64);
constexpr int KIMG = 24 * 384, QIMG = 16 * 384, WIMG = CFG_SHARE ? KIMG : KIMG + QIMG;

__device__ __forceinline__ u32 cvtpk(float lo, float hi) {
  f32x2 f = {lo, hi};
  return __builtin_bit_cast(u32, __builtin_convertvector(f, bf16x2));
}
__device__ __forceinline__ f32x4 mfma(u32x4 a, u32x4 b, f32x4 c) {
  return __builtin_amdgcn_mfma_f32_16x16x32_bf16(__builtin_bit_cast(bf16x8, a), __builtin_bit_cast(bf16x8, b), c, 0, 0, 0);
}
__device__ __forceinline__ float xrow_max(float x) {
  auto s = __builtin_amdgcn_permlane16_swap(__float_as_uint(x), __float_as_uint(x), false, false);
  x = fmaxf(__uint_as_float(s[0]), __uint_as_float(s[1]));
  auto t = __builtin_amdgcn_permlane32_swap(__float_as_uint(x), __float_as_uint(x), false, false);
  return fmaxf(__uint_as_float(t[0]), __uint_as_float(t[1]));
}
__device__ __forceinline__ float xrow_sum(float x) {
  auto s = __builtin_amdgcn_permlane16_swap(__float_as_uint(x), __float_as_uint(x), false, false);
  x = __uint_as_float(s[0]) + __uint_as_float(s[1]);
  auto t = __builtin_amdgcn_permlane32_swap(__float_as_uint(x), __float_as_uint(x), false, false);
  return __uint_as_float(t[0]) + __uint_as_float(t[1]);
}
__device__ __forceinline__ int swz(int R) { return ((R >> 2) & 3) | ((R & 2) << 1); }
#define LD4(r, v, so, sel) ((sel) ? __builtin_amdgcn_raw_buffer_load_b128(r, v, so, CFG_AUXH) : __builtin_amdgcn_raw_buffer_load_b128(r, v, so, CFG_AUXL))
#define LD2(r, v, so, sel) ((sel) ? __builtin_amdgcn_raw_buffer_load_b64(r, v, so, CFG_AUXH) : __builtin_amdgcn_raw_buffer_load_b64(r, v, so, CFG_AUXL))

__global__ void __launch_bounds__(256, CFG_OCC)
swa_kernel(const void* __restrict__ q, const void* __restrict__ k, const void* __restrict__ v, void* __restrict__ out) {
  __shared__ __attribute__((aligned(16))) char lds[4 * WIMG];
  const int lane = threadIdx.x & 63, res = __builtin_amdgcn_readfirstlane(threadIdx.x >> 6);
  const int g = lane >> 4, n = lane & 15, l8 = lane >> 3, c8 = lane & 7;
  const int bid = blockIdx.x;
  const int tile = ((bid >> 3) / CFG_XCD * 8 + (bid & 7)) * CFG_XCD + (bid >> 3) % CFG_XCD;
  const int head = tile >> 5, j0 = (tile & 31) * 16;
  const u32 hbase = (u32)head * HEADB;
  const u32 tbase = hbase + (u32)(j0 * SROWB + res * ROWB);
  const __amdgpu_buffer_rsrc_t rq = __builtin_amdgcn_make_buffer_rsrc((void*)q, 0, TENSORB, 0x00020000);
  const __amdgpu_buffer_rsrc_t rk = __builtin_amdgcn_make_buffer_rsrc((void*)k, 0, TENSORB, 0x00020000);
  const __amdgpu_buffer_rsrc_t rv = __builtin_amdgcn_make_buffer_rsrc((void*)v, 0, TENSORB, 0x00020000);
  const __amdgpu_buffer_rsrc_t ro = __builtin_amdgcn_make_buffer_rsrc(out, 0, TENSORB, 0x00020000);

  u32x4 kk[9], qq[6], va[6];
  u32x2 vb[6];
  const int hrow0 = 4 * (j0 - 8) + res;
  const u32 voLine = (u32)l8 * SROWB + c8 * 16;
  const u32 voHaloK = (u32)max(hrow0 + 4 * max(l8, 1), 0) * ROWB + c8 * 16;
  const u32 voRowA = (u32)g * SROWB + n * 16, voRowB = (u32)g * SROWB + 256 + n * 8;
#pragma unroll
  for (int c = 0; c < 3; ++c) kk[c] = LD4(rk, voHaloK + c * 128, hbase, true);
#pragma unroll
  for (int c = 3; c < 9; ++c) kk[c] = LD4(rk, voLine + (c % 3) * 128, tbase + (c / 3 - 1) * 8 * SROWB, c >= 6);
#pragma unroll
  for (int c = 0; c < 6; ++c) qq[c] = LD4(rq, voLine + (c % 3) * 128, tbase + (c / 3) * 8 * SROWB, false);
#pragma unroll
  for (int c = 0; c < 2; ++c) {
    const u32 ho = (u32)max(hrow0 + 4 * max(4 * c + g, 1), 0) * ROWB;
    va[c] = LD4(rv, ho + n * 16, hbase, true);
    vb[c] = LD2(rv, ho + 256 + n * 8, hbase, true);
  }
#pragma unroll
  for (int c = 2; c < 6; ++c) {
    va[c] = LD4(rv, voRowA, tbase + (c - 2) * 4 * SROWB, c >= 4);
    vb[c] = LD2(rv, voRowB, tbase + (c - 2) * 4 * SROWB, c >= 4);
  }
  __builtin_amdgcn_sched_barrier(0);

  char* kimg = lds + res * WIMG;
  char* qimg = CFG_SHARE ? kimg : kimg + KIMG;
  const int pr = 4 * (n & 3) + (n >> 2);
  const int pr1 = 16 + 4 * (n & 1) + (n >> 2);
#pragma unroll
  for (int c = 0; c < 9; ++c) {
    const int R = 8 * (c / 3) + l8;
    *reinterpret_cast<u32x4*>(kimg + ((c % 3) * 24 + R) * 128 + ((c8 ^ swz(R)) * 16)) = kk[c];
  }
  asm volatile("" ::: "memory");
  u32x4 kf0[6], kf1[6], qf[6];
#pragma unroll
  for (int s = 0; s < 6; ++s) {
    const int ch = 4 * (s & 1) + g;
    kf0[s] = *reinterpret_cast<const u32x4*>(kimg + ((s >> 1) * 24 + pr) * 128 + ((ch ^ swz(pr)) * 16));
    kf1[s] = *reinterpret_cast<const u32x4*>(kimg + ((s >> 1) * 24 + pr1) * 128 + ((ch ^ swz(pr1)) * 16));
  }
  if (CFG_SHARE) asm volatile("" ::: "memory");
#pragma unroll
  for (int c = 0; c < 6; ++c) {
    const int i = 8 * (c / 3) + l8;
    *reinterpret_cast<u32x4*>(qimg + ((c % 3) * 16 + i) * 128 + ((c8 ^ swz(i)) * 16)) = qq[c];
  }
  asm volatile("" ::: "memory");
#pragma unroll
  for (int s = 0; s < 6; ++s) {
    const int ch = 4 * (s & 1) + g;
    qf[s] = *reinterpret_cast<const u32x4*>(qimg + ((s >> 1) * 16 + pr) * 128 + ((ch ^ swz(pr)) * 16));
  }
  f32x4 s0 = {0.f, 0.f, 0.f, 0.f}, s1 = {0.f, 0.f, 0.f, 0.f};
#pragma unroll
  for (int s = 0; s < 6; ++s) { s0 = mfma(kf0[s], qf[s], s0); s1 = mfma(kf1[s], qf[s], s1); }
  constexpr float C2 = 0.07216878364870322f * 1.4426950408889634f;
  constexpr float NEG = -1.0e9f * 1.4426950408889634f;
  const int r0 = (pr + 4 - g) >> 2, rE = (r0 + 1) >> 1, rO = r0 >> 1;
  const float zE = rE == 0 ? s0[0] : rE == 1 ? s0[2] : s1[0];
  const float zO = rO == 0 ? s0[1] : rO == 1 ? s0[3] : s1[1];
  const float tE = fmaf(zE, C2, (j0 == 0 && rE == 0) ? NEG : 0.f), tO = fmaf(zO, C2, (j0 == 0 && rO == 0) ? NEG : 0.f);
  const float m = xrow_max(fmaxf(tE, tO));
  const float eE = __builtin_amdgcn_exp2f(tE - m), eO = __builtin_amdgcn_exp2f(tO - m);
  const float rs = __builtin_amdgcn_rcpf(xrow_sum(eE + eO));
  const u32 pk = cvtpk(eE * rs, eO * rs);
  u32x4 pa;
#pragma unroll
  for (int d = 0; d < 3; ++d) pa[d] = pk & ((rE == d ? 0xffffu : 0u) | (rO == d ? 0xffff0000u : 0u));
  pa[3] = 0u;
  f32x4 acc[12];
#pragma unroll
  for (int e = 0; e < 12; ++e) {
    u32x4 b;
#pragma unroll
    for (int d = 0; d < 3; ++d) {
      const u32 lo = e < 8 ? va[2 * d][e >> 1] : vb[2 * d][(e - 8) >> 1], hi = e < 8 ? va[2 * d + 1][e >> 1] : vb[2 * d + 1][(e - 8) >> 1];
      b[d] = __builtin_amdgcn_perm(hi, lo, (e & 1) ? 0x07060302u : 0x05040100u);
    }
    b[3] = 0u;
    acc[e] = mfma(pa, b, f32x4{0.f, 0.f, 0.f, 0.f});
  }
#pragma unroll
  for (int c = 0; c < 4; ++c) {
    const u32x4 a = {cvtpk(acc[0][c], acc[1][c]), cvtpk(acc[2][c], acc[3][c]), cvtpk(acc[4][c], acc[5][c]), cvtpk(acc[6][c], acc[7][c])};
    const u32x2 b = {cvtpk(acc[8][c], acc[9][c]), cvtpk(acc[10][c], acc[11][c])};
    __builtin_amdgcn_raw_buffer_store_b128(a, ro, voRowA + tbase + c * 4 * SROWB, 0, CFG_AUXO);
    __builtin_amdgcn_raw_buffer_store_b64(b, ro, voRowB + tbase + c * 4 * SROWB, 0, CFG_AUXO);
  }
}

extern "C" void kernel_launch(void* const* d_in, const int* in_sizes, int n_in, void* d_out, int out_size, void* d_ws,
                              size_t ws_size, hipStream_t stream) {
  hipLaunchKernelGGL(swa_kernel, dim3(NBLK), dim3(256), 0, stream, d_in[0], d_in[1], d_in[2], d_out);
}
```

```cpp
#define MODE 4
#include <hip/hip_runtime.h>
#include <hip/hip_bf16.h>
#include <stdint.h>

#define CFG_AUXL 2
#define CFG_AUXH 0
#define CFG_AUXO 16
#define CFG_XCD 32
#define CFG_OCC 2
#define CFG_SHARE 0

typedef unsigned u32;
typedef __attribute__((ext_vector_type(4))) unsigned u32x4;
typedef __attribute__((ext_vector_type(2))) unsigned u32x2;
typedef __attribute__((ext_vector_type(2))) __bf16 bf16x2;
typedef __attribute__((ext_vector_type(8))) __bf16 bf16x8;
typedef __attribute__((ext_vector_type(2))) float f32x2;
typedef __attribute__((ext_vector_type(4))) float f32x4;

constexpr int NH = 32, NS = 2048, ND = 192;
constexpr int ROWB = ND * 2;
constexpr int SROWB = 4 * ROWB;
constexpr int HEADB = NS * ROWB;
constexpr u32 TENSORB = (u32)NH * HEADB;
constexpr int NBLK = NH * (NS / 64);
constexpr int KIMG = 24 * 384, QIMG = 16 * 384, WIMG = CFG_SHARE ? KIMG : KIMG + QIMG;

__device__ __forceinline__ u32 cvtpk(float lo, float hi) {
  f32x2 f = {lo, hi};
  return __builtin_bit_cast(u32, __builtin_convertvector(f, bf16x2));
}
__device__ __forceinline__ f32x4 mfma(u32x4 a, u32x4 b, f32x4 c) {
  return __builtin_amdgcn_mfma_f32_16x16x32_bf16(__builtin_bit_cast(bf16x8, a), __builtin_bit_cast(bf16x8, b), c, 0, 0, 0);
}
__device__ __forceinline__ float xrow_max(float x) {
  auto s = __builtin_amdgcn_permlane16_swap(__float_as_uint(x), __float_as_uint(x), false, false);
  x = fmaxf(__uint_as_float(s[0]), __uint_as_float(s[1]));
  auto t = __builtin_amdgcn_permlane32_swap(__float_as_uint(x), __float_as_uint(x), false, false);
  return fmaxf(__uint_as_float(t[0]), __uint_as_float(t[1]));
}
__device__ __forceinline__ float xrow_sum(float x) {
  auto s = __builtin_amdgcn_permlane16_swap(__float_as_uint(x), __float_as_uint(x), false, false);
  x = __uint_as_float(s[0]) + __uint_as_float(s[1]);
  auto t = __builtin_amdgcn_permlane32_swap(__float_as_uint(x), __float_as_uint(x), false, false);
  return __uint_as_float(t[0]) + __uint_as_float(t[1]);
}
__device__ __forceinline__ int swz(int R) { return ((R >> 2) & 3) | ((R & 2) << 1); }
#define LD4(r, v, so, sel) ((sel) ? __builtin_amdgcn_raw_buffer_load_b128(r, v, so, CFG_AUXH) : __builtin_amdgcn_raw_buffer_load_b128(r, v, so, CFG_AUXL))
#define LD2(r, v, so, sel) ((sel) ? __builtin_amdgcn_raw_buffer_load_b64(r, v, so, CFG_AUXH) : __builtin_amdgcn_raw_buffer_load_b64(r, v, so, CFG_AUXL))

__global__ void __launch_bounds__(256, CFG_OCC)
swa_kernel(const void* __restrict__ q, const void* __restrict__ k, const void* __restrict__ v, void* __restrict__ out, unsigned long long* __restrict__ ws) {
  unsigned long long tA_, tB_, tC_;
  asm volatile("s_memrealtime %0" : "=s"(tA_));
  __builtin_amdgcn_sched_barrier(0);
  __shared__ __attribute__((aligned(16))) char lds[4 * WIMG];
  const int lane = threadIdx.x & 63, res = __builtin_amdgcn_readfirstlane(threadIdx.x >> 6);
  const int g = lane >> 4, n = lane & 15, l8 = lane >> 3, c8 = lane & 7;
  const int bid = blockIdx.x;
  const int tile = ((bid >> 3) / CFG_XCD * 8 + (bid & 7)) * CFG_XCD + (bid >> 3) % CFG_XCD;
  const int head = tile >> 5, j0 = (tile & 31) * 16;
  const u32 hbase = (u32)head * HEADB;
  const u32 tbase = hbase + (u32)(j0 * SROWB + res * ROWB);
  const __amdgpu_buffer_rsrc_t rq = __builtin_amdgcn_make_buffer_rsrc((void*)q, 0, TENSORB, 0x00020000);
  const __amdgpu_buffer_rsrc_t rk = __builtin_amdgcn_make_buffer_rsrc((void*)k, 0, TENSORB, 0x00020000);
  const __amdgpu_buffer_rsrc_t rv = __builtin_amdgcn_make_buffer_rsrc((void*)v, 0, TENSORB, 0x00020000);
  const __amdgpu_buffer_rsrc_t ro = __builtin_amdgcn_make_buffer_rsrc(out, 0, TENSORB, 0x00020000);

  u32x4 kk[9], qq[6], va[6];
  u32x2 vb[6];
  const int hrow0 = 4 * (j0 - 8) + res;
  const u32 voLine = (u32)l8 * SROWB + c8 * 16;
  const u32 voHaloK = (u32)max(hrow0 + 4 * max(l8, 1), 0) * ROWB + c8 * 16;
  const u32 voRowA = (u32)g * SROWB + n * 16, voRowB = (u32)g * SROWB + 256 + n * 8;
#pragma unroll
  for (int c = 0; c < 3; ++c) kk[c] = LD4(rk, voHaloK + c * 128, hbase, true);
#pragma unroll
  for (int c = 3; c < 9; ++c) kk[c] = LD4(rk, voLine + (c % 3) * 128, tbase + (c / 3 - 1) * 8 * SROWB, c >= 6);
#pragma unroll
  for (int c = 0; c < 6; ++c) qq[c] = LD4(rq, voLine + (c % 3) * 128, tbase + (c / 3) * 8 * SROWB, false);
#pragma unroll
  for (int c = 0; c < 2; ++c) {
    const u32 ho = (u32)max(hrow0 + 4 * max(4 * c + g, 1), 0) * ROWB;
    va[c] = LD4(rv, ho + n * 16, hbase, true);
    vb[c] = LD2(rv, ho + 256 + n * 8, hbase, true);
  }
#pragma unroll
  for (int c = 2; c < 6; ++c) {
    va[c] = LD4(rv, voRowA, tbase + (c - 2) * 4 * SROWB, c >= 4);
    vb[c] = LD2(rv, voRowB, tbase + (c - 2) * 4 * SROWB, c >= 4);
  }
  __builtin_amdgcn_sched_barrier(0);
  asm volatile("s_memrealtime %0\n\ts_waitcnt lgkmcnt(0)" : "=s"(tB_));
  asm volatile("s_waitcnt vmcnt(26)\n\ts_memrealtime %0\n\ts_waitcnt lgkmcnt(0)" : "=s"(tC_));
  __builtin_amdgcn_sched_barrier(0);

  char* kimg = lds + res * WIMG;
  char* qimg = CFG_SHARE ? kimg : kimg + KIMG;
  const int pr = 4 * (n & 3) + (n >> 2);
  const int pr1 = 16 + 4 * (n & 1) + (n >> 2);
#pragma unroll
  for (int c = 0; c < 9; ++c) {
    const int R = 8 * (c / 3) + l8;
    *reinterpret_cast<u32x4*>(kimg + ((c % 3) * 24 + R) * 128 + ((c8 ^ swz(R)) * 16)) = kk[c];
  }
  asm volatile("" ::: "memory");
  u32x4 kf0[6], kf1[6], qf[6];
#pragma unroll
  for (int s = 0; s < 6; ++s) {
    const int ch = 4 * (s & 1) + g;
    kf0[s] = *reinterpret_cast<const u32x4*>(kimg + ((s >> 1) * 24 + pr) * 128 + ((ch ^ swz(pr)) * 16));
    kf1[s] = *reinterpret_cast<const u32x4*>(kimg + ((s >> 1) * 24 + pr1) * 128 + ((ch ^ swz(pr1)) * 16));
  }
  if (CFG_SHARE) asm volatile("" ::: "memory");
#pragma unroll
  for (int c = 0; c < 6; ++c) {
    const int i = 8 * (c / 3) + l8;
    *reinterpret_cast<u32x4*>(qimg + ((c % 3) * 16 + i) * 128 + ((c8 ^ swz(i)) * 16)) = qq[c];
  }
  asm volatile("" ::: "memory");
#pragma unroll
  for (int s = 0; s < 6; ++s) {
    const int ch = 4 * (s & 1) + g;
    qf[s] = *reinterpret_cast<const u32x4*>(qimg + ((s >> 1) * 16 + pr) * 128 + ((ch ^ swz(pr)) * 16));
  }
  f32x4 s0 = {0.f, 0.f, 0.f, 0.f}, s1 = {0.f, 0.f, 0.f, 0.f};
#pragma unroll
  for (int s = 0; s < 6; ++s) { s0 = mfma(kf0[s], qf[s], s0); s1 = mfma(kf1[s], qf[s], s1); }
  constexpr float C2 = 0.07216878364870322f * 1.4426950408889634f;
  constexpr float NEG = -1.0e9f * 1.4426950408889634f;
  const int r0 = (pr + 4 - g) >> 2, rE = (r0 + 1) >> 1, rO = r0 >> 1;
  const float zE = rE == 0 ? s0[0] : rE == 1 ? s0[2] : s1[0];
  const float zO = rO == 0 ? s0[1] : rO == 1 ? s0[3] : s1[1];
  const float tE = fmaf(zE, C2, (j0 == 0 && rE == 0) ? NEG : 0.f), tO = fmaf(zO, C2, (j0 == 0 && rO == 0) ? NEG : 0.f);
  const float m = xrow_max(fmaxf(tE, tO));
  const float eE = __builtin_amdgcn_exp2f(tE - m), eO = __builtin_amdgcn_exp2f(tO - m);
  const float rs = __builtin_amdgcn_rcpf(xrow_sum(eE + eO));
  const u32 pk = cvtpk(eE * rs, eO * rs);
  u32x4 pa;
#pragma unroll
  for (int d = 0; d < 3; ++d) pa[d] = pk & ((rE == d ? 0xffffu : 0u) | (rO == d ? 0xffff0000u : 0u));
  pa[3] = 0u;
  f32x4 acc[12];
#pragma unroll
  for (int e = 0; e < 12; ++e) {
    u32x4 b;
#pragma unroll
    for (int d = 0; d < 3; ++d) {
      const u32 lo = e < 8 ? va[2 * d][e >> 1] : vb[2 * d][(e - 8) >> 1], hi = e < 8 ? va[2 * d + 1][e >> 1] : vb[2 * d + 1][(e - 8) >> 1];
      b[d] = __builtin_amdgcn_perm(hi, lo, (e & 1) ? 0x07060302u : 0x05040100u);
    }
    b[3] = 0u;
    acc[e] = mfma(pa, b, f32x4{0.f, 0.f, 0.f, 0.f});
  }
#pragma unroll
  for (int c = 0; c < 4; ++c) {
    const u32x4 a = {cvtpk(acc[0][c], acc[1][c]), cvtpk(acc[2][c], acc[3][c]), cvtpk(acc[4][c], acc[5][c]), cvtpk(acc[6][c], acc[7][c])};
    const u32x2 b = {cvtpk(acc[8][c], acc[9][c]), cvtpk(acc[10][c], acc[11][c])};
    __builtin_amdgcn_raw_buffer_store_b128(a, ro, voRowA + tbase + c * 4 * SROWB, 0, CFG_AUXO);
    __builtin_amdgcn_raw_buffer_store_b64(b, ro, voRowB + tbase + c * 4 * SROWB, 0, CFG_AUXO);
  }
  asm volatile("s_waitcnt vmcnt(0)" ::: "memory");
  const unsigned long long tD_ = __builtin_amdgcn_s_memrealtime();
  if (lane == 0) { unsigned long long* w = ws + 4 * (blockIdx.x * 4 + res); w[0] = tA_; w[1] = tB_; w[2] = tC_; w[3] = tD_; }
}

__global__ void spin_k(const unsigned long long* ws) {
  __shared__ unsigned long long a[1024], b[1024];
  unsigned long long lo = ~0ull, hi = 0, x0 = ~0ull;
  for (int r = 0; r < 4; ++r) {
    const unsigned long long* w = ws + 4 * (threadIdx.x * 4 + r);
    unsigned long long l, h;
    if (MODE == 0) { l = w[0]; h = w[3]; }
    else if (MODE == 1) { l = w[0]; h = ~w[1]; }
    else if (MODE == 2) { l = w[0]; h = ~w[2]; }
    else if (MODE == 3) { l = w[0]; h = ~w[3]; }
    else if (MODE == 4) { l = w[0] >> 62; h = w[0] >> 62; }
    else { l = w[0]; h = threadIdx.x < 256 ? w[2] : 0; }
    lo = l < lo ? l : lo; hi = h > hi ? h : hi;
  }
  a[threadIdx.x] = lo; b[threadIdx.x] = hi; __syncthreads();
  for (int st = 512; st > 0; st >>= 1) {
    if (threadIdx.x < st) { unsigned long long x = a[threadIdx.x + st], y = b[threadIdx.x + st];
      if (x < a[threadIdx.x]) a[threadIdx.x] = x; if (y > b[threadIdx.x]) b[threadIdx.x] = y; }
    __syncthreads();
  }
  if (threadIdx.x == 0) {
    unsigned long long hh = (MODE >= 1 && MODE <= 3) ? ~b[0] : b[0];
    unsigned long long d = hh - a[0]; if (d > 10000) d = 10000;
    const unsigned long long tend = __builtin_amdgcn_s_memrealtime() + 4 * d;
    while (__builtin_amdgcn_s_memrealtime() < tend) __builtin_amdgcn_s_sleep(1);
  }
}
extern "C" void kernel_launch(void* const* d_in, const int* in_sizes, int n_in, void* d_out, int out_size, void* d_ws,
                              size_t ws_size, hipStream_t stream) {
  hipLaunchKernelGGL(swa_kernel, dim3(NBLK), dim3(256), 0, stream, d_in[0], d_in[1], d_in[2], d_out, (unsigned long long*)d_ws);
  hipLaunchKernelGGL(spin_k, dim3(1), dim3(1024), 0, stream, (const unsigned long long*)d_ws);
}
```

```cpp
#include <hip/hip_runtime.h>
#include <hip/hip_bf16.h>
#include <stdint.h>

typedef unsigned u32;
typedef unsigned long long u64;
typedef __attribute__((ext_vector_type(4))) unsigned u32x4;
typedef __attribute__((ext_vector_type(2))) unsigned u32x2;
typedef __attribute__((ext_vector_type(2))) __bf16 bf16x2;
typedef __attribute__((ext_vector_type(8))) __bf16 bf16x8;
typedef __attribute__((ext_vector_type(2))) float f32x2;
typedef __attribute__((ext_vector_type(4))) float f32x4;

constexpr int NH = 32, NS = 2048, ND = 192;
constexpr int ROWB = ND * 2;
constexpr int SROWB = 4 * ROWB;
constexpr int HEADB = NS * ROWB;
constexpr u32 TENSORB = (u32)NH * HEADB;
constexpr int NBLK = 256, WPB = 16;
constexpr int WIMG = 24 * ROWB;
constexpr int CODEB = 3392;
constexpr int AUX_NT = 2;
constexpr int T_MARGIN = 40;
constexpr int T_FALLBACK = 180;
constexpr int T_BACK = 384;
constexpr int T_CAP = 400;

__device__ __forceinline__ u32 cvtpk(float lo, float hi) {
  f32x2 f = {lo, hi};
  return __builtin_bit_cast(u32, __builtin_convertvector(f, bf16x2));
}
__device__ __forceinline__ f32x4 mfma(u32x4 a, u32x4 b, f32x4 c) {
  return __builtin_amdgcn_mfma_f32_16x16x32_bf16(__builtin_bit_cast(bf16x8, a), __builtin_bit_cast(bf16x8, b), c, 0, 0, 0);
}
__device__ __forceinline__ float xrow_max(float x) {
  auto s = __builtin_amdgcn_permlane16_swap(__float_as_uint(x), __float_as_uint(x), false, false);
  x = fmaxf(__uint_as_float(s[0]), __uint_as_float(s[1]));
  auto t = __builtin_amdgcn_permlane32_swap(__float_as_uint(x), __float_as_uint(x), false, false);
  return fmaxf(__uint_as_float(t[0]), __uint_as_float(t[1]));
}
__device__ __forceinline__ float xrow_sum(float x) {
  auto s = __builtin_amdgcn_permlane16_swap(__float_as_uint(x), __float_as_uint(x), false, false);
  x = __uint_as_float(s[0]) + __uint_as_float(s[1]);
  auto t = __builtin_amdgcn_permlane32_swap(__float_as_uint(x), __float_as_uint(x), false, false);
  return __uint_as_float(t[0]) + __uint_as_float(t[1]);
}
__device__ __forceinline__ int swz(int R) { return ((R >> 2) & 3) | ((R & 2) << 1); }
__device__ __forceinline__ u32 touch(u64 p) { return *(const __attribute__((address_space(4))) u32*)p; }
__device__ __forceinline__ u64 now() { return __builtin_amdgcn_s_memrealtime(); }
#define LD4(r, v, so, shared) ((shared) ? __builtin_amdgcn_raw_buffer_load_b128(r, v, so, 0) : __builtin_amdgcn_raw_buffer_load_b128(r, v, so, AUX_NT))
#define LD2(r, v, so, shared) ((shared) ? __builtin_amdgcn_raw_buffer_load_b64(r, v, so, 0) : __builtin_amdgcn_raw_buffer_load_b64(r, v, so, AUX_NT))

__global__ void __launch_bounds__(64 * WPB, 4)
swa_kernel(const void* __restrict__ q, const void* __restrict__ k, const void* __restrict__ v, void* __restrict__ out, u64* __restrict__ hint) {
  __shared__ __attribute__((aligned(16))) char lds[WPB * WIMG + 16];
  __builtin_amdgcn_sched_barrier(0);
  const u64 tst = now();
  const int lane = threadIdx.x & 63, wid = __builtin_amdgcn_readfirstlane(threadIdx.x >> 6), res = wid & 3;
  u32 cpf = 0;
  if (wid == 0 && blockIdx.x < 32) {
    const __amdgpu_buffer_rsrc_t rc = __builtin_amdgcn_make_buffer_rsrc((void*)(__builtin_amdgcn_s_getpc() & ~255ull), 0, CODEB, 0x00020000);
    cpf = __builtin_amdgcn_raw_buffer_load_b32(rc, lane * 128, 0, 0);
  }
  const int g = lane >> 4, n = lane & 15, l8 = lane >> 3, c8 = lane & 7;
  const int head = (blockIdx.x & 7) + 8 * (wid >> 2), j0 = (blockIdx.x >> 3) * 16;
  const u32 hbase = (u32)head * HEADB;
  const u32 tbase = hbase + (u32)(j0 * SROWB + res * ROWB);
  u32x4 kk[9], qq[6], va[6];
  u32x2 vb[6];
  const int hrow0 = 4 * (j0 - 8) + res;
  u32 voLine = (u32)l8 * SROWB + c8 * 16;
  u32 voHaloK = (u32)max(hrow0 + 4 * max(l8, 1), 0) * ROWB + c8 * 16;
  const u32 voRowA = (u32)g * SROWB + n * 16, voRowB = (u32)g * SROWB + 256 + n * 8;
  const u32 tgr = (u32)(j0 * SROWB);
  u32 toff0 = (blockIdx.x & 7) * HEADB + tgr, hb = hbase, tb = tbase;
  asm volatile("" : "+v"(voLine), "+v"(voHaloK), "+s"(toff0), "+s"(hb), "+s"(tb));
  __builtin_amdgcn_sched_barrier(0);
  const __amdgpu_buffer_rsrc_t rq = __builtin_amdgcn_make_buffer_rsrc((void*)q, 0, TENSORB, 0x00020000);
  const __amdgpu_buffer_rsrc_t rk = __builtin_amdgcn_make_buffer_rsrc((void*)k, 0, TENSORB, 0x00020000);
  const __amdgpu_buffer_rsrc_t rv = __builtin_amdgcn_make_buffer_rsrc((void*)v, 0, TENSORB, 0x00020000);
  const __amdgpu_buffer_rsrc_t ro = __builtin_amdgcn_make_buffer_rsrc(out, 0, TENSORB, 0x00020000);
  const u64 tp01 = (res & 1) ? (u64)q : (u64)k, tp23 = (res & 1) ? (u64)out : (u64)v;
  u32 tch0 = 0, tch1 = 0;
#pragma unroll
  for (int c = 0; c < 9; ++c) {
    if (c < 3) kk[c] = LD4(rk, voHaloK + c * 128, hb, true);
    else kk[c] = LD4(rk, voLine + (c % 3) * 128, tb + (c / 3 - 1) * 8 * SROWB, c >= 6);
    if (c == 3) {
      __builtin_amdgcn_sched_barrier(0);
      tch0 = touch(((res & 1) ? (u64)q : (u64)v) + toff0);
      __builtin_amdgcn_sched_barrier(0);
    }
    if (c == 8) {
      __builtin_amdgcn_sched_barrier(0);
      tch1 = touch(((res & 2) ? tp23 : tp01) + (hbase + tgr + ((wid < 12 && res < 3) ? 8 * HEADB : 0)));
      __builtin_amdgcn_sched_barrier(0);
    }
  }
#pragma unroll
  for (int c = 0; c < 6; ++c) qq[c] = LD4(rq, voLine + (c % 3) * 128, tbase + (c / 3) * 8 * SROWB, false);
#pragma unroll
  for (int c = 0; c < 2; ++c) {
    const u32 ho = (u32)max(hrow0 + 4 * max(4 * c + g, 1), 0) * ROWB;
    va[c] = LD4(rv, ho + n * 16, hbase, true);
    vb[c] = LD2(rv, ho + 256 + n * 8, hbase, true);
  }
#pragma unroll
  for (int c = 2; c < 6; ++c) {
    va[c] = LD4(rv, voRowA, tbase + (c - 2) * 4 * SROWB, c >= 4);
    vb[c] = LD2(rv, voRowB, tbase + (c - 2) * 4 * SROWB, c >= 4);
  }
  __builtin_amdgcn_sched_barrier(0);
  u32 tbar = 0;
  if (wid >= 12) { __builtin_amdgcn_s_barrier(); tbar = (u32)now(); }
  __builtin_amdgcn_sched_barrier(0);

  char* img = lds + wid * WIMG;
  const int pr = 4 * (n & 3) + (n >> 2);
  const int pr1 = 16 + 4 * (n & 1) + (n >> 2);
#pragma unroll
  for (int c = 0; c < 9; ++c) {
    const int R = 8 * (c / 3) + l8;
    *reinterpret_cast<u32x4*>(img + ((c % 3) * 24 + R) * 128 + ((c8 ^ swz(R)) * 16)) = kk[c];
  }
  asm volatile("" ::: "memory");
  u32x4 kf0[6], kf1[6], qf[6];
#pragma unroll
  for (int s = 0; s < 6; ++s) {
    const int ch = 4 * (s & 1) + g;
    kf0[s] = *reinterpret_cast<const u32x4*>(img + ((s >> 1) * 24 + pr) * 128 + ((ch ^ swz(pr)) * 16));
    kf1[s] = *reinterpret_cast<const u32x4*>(img + ((s >> 1) * 24 + pr1) * 128 + ((ch ^ swz(pr1)) * 16));
  }
  asm volatile("" ::: "memory");
#pragma unroll
  for (int c = 0; c < 6; ++c) {
    const int i = 8 * (c / 3) + l8;
    *reinterpret_cast<u32x4*>(img + ((c % 3) * 16 + i) * 128 + ((c8 ^ swz(i)) * 16)) = qq[c];
  }
  asm volatile("" ::: "memory");
#pragma unroll
  for (int s = 0; s < 6; ++s) {
    const int ch = 4 * (s & 1) + g;
    qf[s] = *reinterpret_cast<const u32x4*>(img + ((s >> 1) * 16 + pr) * 128 + ((ch ^ swz(pr)) * 16));
  }
  asm volatile("" :: "s"(tch0), "s"(tch1), "v"(cpf));
  f32x4 s0 = {0.f, 0.f, 0.f, 0.f}, s1 = {0.f, 0.f, 0.f, 0.f};
#pragma unroll
  for (int s = 0; s < 6; ++s) { s0 = mfma(kf0[s], qf[s], s0); s1 = mfma(kf1[s], qf[s], s1); }
  constexpr float C2 = 0.07216878364870322f * 1.4426950408889634f;
  constexpr float NEG = -1.0e9f * 1.4426950408889634f;
  const int r0 = (pr + 4 - g) >> 2, rE = (r0 + 1) >> 1, rO = r0 >> 1;
  const float zE = rE == 0 ? s0[0] : rE == 1 ? s0[2] : s1[0];
  const float zO = rO == 0 ? s0[1] : rO == 1 ? s0[3] : s1[1];
  const float tE = fmaf(zE, C2, (j0 == 0 && rE == 0) ? NEG : 0.f), tO = fmaf(zO, C2, (j0 == 0 && rO == 0) ? NEG : 0.f);
  const float m = xrow_max(fmaxf(tE, tO));
  const float eE = __builtin_amdgcn_exp2f(tE - m), eO = __builtin_amdgcn_exp2f(tO - m);
  const float rinv = __builtin_amdgcn_rcpf(xrow_sum(eE + eO));
  const u32 pk = cvtpk(eE * rinv, eO * rinv);
  u32x4 pa;
#pragma unroll
  for (int d = 0; d < 3; ++d) pa[d] = pk & ((rE == d ? 0xffffu : 0u) | (rO == d ? 0xffff0000u : 0u));
  pa[3] = 0u;
  f32x4 acc[12];
#pragma unroll
  for (int e = 0; e < 12; ++e) {
    u32x4 b;
#pragma unroll
    for (int d = 0; d < 3; ++d) {
      const u32 lo = e < 8 ? va[2 * d][e >> 1] : vb[2 * d][(e - 8) >> 1], hi = e < 8 ? va[2 * d + 1][e >> 1] : vb[2 * d + 1][(e - 8) >> 1];
      b[d] = __builtin_amdgcn_perm(hi, lo, (e & 1) ? 0x07060302u : 0x05040100u);
    }
    b[3] = 0u;
    acc[e] = mfma(pa, b, f32x4{0.f, 0.f, 0.f, 0.f});
  }
  u32x4 oa[4];
  u32x2 ob[4];
#pragma unroll
  for (int c = 0; c < 4; ++c) {
    oa[c] = u32x4{cvtpk(acc[0][c], acc[1][c]), cvtpk(acc[2][c], acc[3][c]), cvtpk(acc[4][c], acc[5][c]), cvtpk(acc[6][c], acc[7][c])};
    ob[c] = u32x2{cvtpk(acc[8][c], acc[9][c]), cvtpk(acc[10][c], acc[11][c])};
    asm volatile("" : "+v"(oa[c]), "+v"(ob[c]));
  }

  volatile __attribute__((address_space(3))) u64* lt = (volatile __attribute__((address_space(3))) u64*)(lds + WPB * WIMG);
  if (wid == 0) {
    if (lane == 0) lt[0] = now();
  } else if (wid == 4) {
    if (((blockIdx.x >> 3) & 3) == 1) {
      const u64 t1 = now(), dt = t1 - lt[0];
      if (lane < 8 && dt <= t1 - tst && dt < 4096)
        __hip_atomic_store(hint + lane * 512 + (blockIdx.x & 7) * 8 + (blockIdx.x >> 5), (t1 << 12) | dt, __ATOMIC_RELAXED, __HIP_MEMORY_SCOPE_AGENT);
    }
  } else if (wid == 8) {
    const u64 h = __hip_atomic_load(hint + (blockIdx.x & 7) * 512 + lane, __ATOMIC_RELAXED, __HIP_MEMORY_SCOPE_AGENT);
    asm volatile("" :: "v"(h));
    const u64 t2 = now() - T_BACK, age = ((t2 + T_BACK - (h >> 12)) << 12) >> 12;
    const u32 dp = 2 * ((u32)h & 4095u) - (u32)age + T_BACK;
    const u32 d = (age <= t2 + T_BACK - tst && dp < 1024u) ? dp : 1024u;
    const int cnt = __popcll(__ballot(d < 1024u));
    int x = d < 1024u ? (int)d : 0;
    x += __builtin_amdgcn_mov_dpp(x, 0xB1, 0xf, 0xf, true);
    x += __builtin_amdgcn_mov_dpp(x, 0x4E, 0xf, 0xf, true);
    x += __builtin_amdgcn_mov_dpp(x, 0x141, 0xf, 0xf, true);
    x += __builtin_amdgcn_mov_dpp(x, 0x140, 0xf, 0xf, true);
    const int sum = __builtin_amdgcn_readlane(x, 0) + __builtin_amdgcn_readlane(x, 16) + __builtin_amdgcn_readlane(x, 32) + __builtin_amdgcn_readlane(x, 48);
    const u32 mean = (u32)((float)sum * __builtin_amdgcn_rcpf((float)max(cnt, 1)));
    if (lane == 0) lt[1] = t2 + (cnt >= 32 ? mean + T_MARGIN : T_BACK + T_FALLBACK);
    asm volatile("s_waitcnt lgkmcnt(0)" ::: "memory");
  }
  if (wid < 12) { __builtin_amdgcn_s_barrier(); tbar = (u32)now(); }
  asm volatile("" ::: "memory");
  {
    const int w = min((int)(__builtin_amdgcn_readfirstlane((u32)lt[1]) - tbar), T_CAP);
    while ((int)((u32)now() - tbar) < w) __builtin_amdgcn_s_sleep(1);
  }
#pragma unroll
  for (int c = 0; c < 4; ++c) {
    __builtin_amdgcn_raw_buffer_store_b128(oa[c], ro, voRowA + tbase + c * 4 * SROWB, 0, AUX_NT);
    __builtin_amdgcn_raw_buffer_store_b64(ob[c], ro, voRowB + tbase + c * 4 * SROWB, 0, AUX_NT);
  }
}

extern "C" void kernel_launch(void* const* d_in, const int* in_sizes, int n_in, void* d_out, int out_size, void* d_ws,
                              size_t ws_size, hipStream_t stream) {
  hipLaunchKernelGGL(swa_kernel, dim3(NBLK), dim3(64 * WPB), 0, stream, d_in[0], d_in[1], d_in[2], d_out, (u64*)d_ws);
}
```
